```python
import torch

def _softmax(x, axis=-1):
    return torch.softmax(x, dim=axis)
def _gelu(x):
    return torch.nn.functional.gelu(x, approximate="tanh")
def _silu(x):
    return torch.nn.functional.silu(x)
def _sigmoid(x):
    return torch.sigmoid(x)
def _layernorm(x, axis=-1, eps=1e-5):
    m = x.mean(dim=axis, keepdim=True)
    v = x.var(dim=axis, keepdim=True, unbiased=False)
    return (x - m) / torch.sqrt(v + eps)
def _scatter_add(tgt, idx, src, axis=0):
    return tgt.clone().index_add_(axis, idx, src)
def _segment_sum(src, seg, n):
    out = torch.zeros((n,) + src.shape[1:], dtype=src.dtype)
    return out.index_add_(0, seg, src)
def _pad(x, pw):
    flat = tuple(v for pair in reversed(pw) for v in pair)
    return torch.nn.functional.pad(x, flat)

kernel_name = 'standalone__softmax_full_bidir__sampling'

def setup_inputs():

    _w = (lambda a: (lambda t: t.to(torch.bfloat16) if t.is_floating_point() else t)(torch.as_tensor(a)))
    B, H, Sq, Skv, Dh = 16, 32, 4, 4096, 128
    return {
        "q": _w((lambda *s: torch.randn(*s, dtype=torch.bfloat16) * 0.8)(B, H, Sq, Dh)),
        "k": _w((lambda *s: torch.randn(*s, dtype=torch.bfloat16) * 0.8)(B, H, Skv, Dh)),
        "v": _w((lambda *s: torch.randn(*s, dtype=torch.bfloat16) * 0.05)(B, H, Skv, Dh)),
    }

def reference(q, k, v):
    scale = q.shape[-1] ** -0.5
    scores = torch.einsum('bhqd,bhkd->bhqk', q, k) * scale
    attn = _softmax(scores, axis=-1)
    return torch.einsum('bhqk,bhkd->bhqd', attn, v)
```

```cpp
#define DIAG 1
#include <stdio.h>
#include <hip/hip_runtime.h>
#include <hip/hip_bf16.h>
#include <stdint.h>
#include <type_traits>

typedef __attribute__((ext_vector_type(4))) unsigned u32x4;
typedef __attribute__((ext_vector_type(2))) unsigned u32x2;
typedef __attribute__((ext_vector_type(4))) float f32x4;
typedef __attribute__((ext_vector_type(8))) __bf16 bf16x8;
typedef __attribute__((ext_vector_type(4))) short s16x4;

constexpr int SKV = 4096, D = 128, SQ = 4, NBH = 512;
#define PACE 13800
#define SLIP 10
#define RING 8
#define LEAD 0
#define AUX 2
constexpr int DT = PACE * 256 / 1024;
constexpr int NBLK = 512 / RING;

template <int CTRL> __device__ __forceinline__ float dpp(float x) {
  return __builtin_bit_cast(float, __builtin_amdgcn_mov_dpp(__builtin_bit_cast(int, x), CTRL, 0xf, 0xf, true));
}
template <int CTRL> __device__ __forceinline__ unsigned dppu(unsigned x) {
  return (unsigned)__builtin_amdgcn_mov_dpp((int)x, CTRL, 0xf, 0xf, true);
}
__device__ __forceinline__ float all_max(float x) {
  x = fmaxf(x, dpp<0xB1>(x)); x = fmaxf(x, dpp<0x4E>(x));
  auto s = __builtin_amdgcn_permlane16_swap(__float_as_uint(x), __float_as_uint(x), false, false);
  x = fmaxf(__uint_as_float(s[0]), __uint_as_float(s[1]));
  auto t = __builtin_amdgcn_permlane32_swap(__float_as_uint(x), __float_as_uint(x), false, false);
  return fmaxf(__uint_as_float(t[0]), __uint_as_float(t[1]));
}
__device__ __forceinline__ float all_sum(float x) {
  x += dpp<0xB1>(x); x += dpp<0x4E>(x);
  auto s = __builtin_amdgcn_permlane16_swap(__float_as_uint(x), __float_as_uint(x), false, false);
  x = __uint_as_float(s[0]) + __uint_as_float(s[1]);
  auto t = __builtin_amdgcn_permlane32_swap(__float_as_uint(x), __float_as_uint(x), false, false);
  return __uint_as_float(t[0]) + __uint_as_float(t[1]);
}
__device__ __forceinline__ unsigned cvtpk(float lo, float hi) {
  typedef __attribute__((ext_vector_type(2))) float f32x2;
  typedef __attribute__((ext_vector_type(2))) __bf16 bf16x2;
  f32x2 v = {lo, hi};
  return __builtin_bit_cast(unsigned, __builtin_convertvector(v, bf16x2));
}
__device__ __forceinline__ unsigned clk() { return (unsigned)__builtin_amdgcn_s_memrealtime() << 8; }
#define DIAG_ARG , unsigned* __restrict__ dg
#define DG(i, v) do { const unsigned v_ = (v); if (lane == 0) dg[(b * 4 + w) * 8 + (i)] = v_; } while (0)
__device__ __forceinline__ void pace(unsigned& tnext) {
  int d = (int)(clk() - tnext);
  if (d > SLIP * 256) tnext += d - SLIP * 256;
  while (d < 0) { __builtin_amdgcn_s_sleep(1); d = (int)(clk() - tnext); }
  tnext += DT;
}
__device__ __forceinline__ int ldoff(int i) { return ((i & 15) << 11) + ((i >> 4) << 16); }

__global__ void __launch_bounds__(256, 1) attn_kernel(const __hip_bfloat16* __restrict__ Q, const __hip_bfloat16* __restrict__ K,
                                                      const __hip_bfloat16* __restrict__ V, __hip_bfloat16* __restrict__ O DIAG_ARG) {
  const unsigned tk0 = clk();
  __shared__ float sm[4][128][64];
  constexpr float C2 = 0.088388347648318440f * 1.4426950408889634f;
  const int b = blockIdx.x;
  const int tid = threadIdx.x, lane = tid & 63, w = __builtin_amdgcn_readfirstlane(tid >> 6);
  const int g = lane >> 4, r4 = (lane >> 2) & 3, j4 = lane & 3;
  const char* kp = reinterpret_cast<const char*>(K) + (size_t)b * (2 * SKV * D * 2);
  const char* vp = reinterpret_cast<const char*>(V) + (size_t)b * (2 * SKV * D * 2);
  const int woff = (w & 1) * 1024 + (w >> 1) * 32768;
  const int qoff = r4 * 256 + g * 64 + j4 * 16;
  const int koff = qoff + woff, voff = lane * 16 + woff;
  __amdgpu_buffer_rsrc_t rk = __builtin_amdgcn_make_buffer_rsrc((void*)kp, 0, 2 * SKV * D * 2, 0x00020000);
  __amdgpu_buffer_rsrc_t rv = __builtin_amdgcn_make_buffer_rsrc((void*)vp, 0, 2 * SKV * D * 2, 0x00020000);
  unsigned tnext = clk() + w * (DT / 4) - LEAD * DT;
  const unsigned ts0 = tnext;
  const char* qp = reinterpret_cast<const char*>(Q) + (size_t)b * (2 * SQ * D * 2) + qoff;
  u32x4 q = *reinterpret_cast<const u32x4*>(qp);
  const u32x4 q1 = *reinterpret_cast<const u32x4*>(qp + SQ * D * 2);
  __builtin_amdgcn_sched_barrier(0);
  u32x4 r[RING];
#pragma unroll
  for (int s = 0; s < RING; ++s) {
    pace(tnext);
    r[s] = __builtin_amdgcn_raw_buffer_load_b128(rk, koff, ldoff(s), AUX);
    __builtin_amdgcn_sched_barrier(0);
  }
  const unsigned ts1 = tnext;
  float* sc = &sm[w][0][lane];
  float mx = -INFINITY, mx0 = 0.f;
  for (int blk = 0; blk < NBLK; ++blk) {
    if (blk == NBLK / 2) { mx0 = mx; mx = -INFINITY; q = q1; }
    const bool last = blk == NBLK - 1;
    __amdgpu_buffer_rsrc_t rs = __builtin_amdgcn_make_buffer_rsrc((void*)(last ? vp : kp), 0, 2 * SKV * D * 2, 0x00020000);
    const int lo = last ? voff : koff;
    const int nb = last ? 0 : (blk + 1) * RING;
    float y[4];
#pragma unroll
    for (int s = 0; s < RING; ++s) {
      const f32x4 z = {0.f, 0.f, 0.f, 0.f};
      const f32x4 d = __builtin_amdgcn_mfma_f32_16x16x32_bf16(__builtin_bit_cast(bf16x8, r[s]), __builtin_bit_cast(bf16x8, q), z, 0, 0, 0);
      __builtin_amdgcn_sched_barrier(0);
      pace(tnext);
      r[s] = __builtin_amdgcn_raw_buffer_load_b128(rs, lo, ldoff(nb + s), AUX);
      __builtin_amdgcn_sched_barrier(0);
      y[s & 3] = ((dpp<0x00>(d[0]) + dpp<0x55>(d[1])) + dpp<0xAA>(d[2])) + dpp<0xFF>(d[3]);
      if ((s & 3) == 3) {
        const float yd = (j4 & 2) ? ((j4 & 1) ? y[3] : y[2]) : ((j4 & 1) ? y[1] : y[0]);
        mx = fmaxf(mx, yd);
        sc[(blk * (RING / 4) + (s >> 2)) * 64] = yd;
      }
    }
  }
  const unsigned ts2 = tnext;
  const float m0 = all_max(mx0) * C2, m1 = all_max(mx) * C2;
  const unsigned mk0 = j4 == 0 ? 0x0000ffffu : j4 == 1 ? 0xffff0000u : 0u;
  const unsigned mk1 = j4 == 2 ? 0x0000ffffu : j4 == 3 ? 0xffff0000u : 0u;
  f32x4 a0 = {0.f, 0.f, 0.f, 0.f}, a1 = a0, h0a0 = a0, h0a1 = a0;
  float l = 0.f, l0 = 0.f, m = m0;
  float yn[RING / 4];
#pragma unroll
  for (int u = 0; u < RING / 4; ++u) yn[u] = sc[u * 64];
  auto vblock = [&](int blk, auto LOAD) {
    unsigned pk[RING / 4];
#pragma unroll
    for (int u = 0; u < RING / 4; ++u) { const float p = __builtin_amdgcn_exp2f(fmaf(yn[u], C2, -m)); l += p; pk[u] = cvtpk(p, p); }
    if (LOAD.value) {
#pragma unroll
      for (int u = 0; u < RING / 4; ++u) yn[u] = sc[((blk + 1) * (RING / 4) + u) * 64];
    }
#pragma unroll
    for (int s = 0; s < RING; ++s) {
      const unsigned pb = (s & 3) == 0 ? dppu<0x00>(pk[s >> 2]) : (s & 3) == 1 ? dppu<0x55>(pk[s >> 2]) : (s & 3) == 2 ? dppu<0xAA>(pk[s >> 2]) : dppu<0xFF>(pk[s >> 2]);
      const u32x2 bb = {pb & mk0, pb & mk1};
      const u32x2 v0 = {r[s][0], r[s][1]}, v1 = {r[s][2], r[s][3]};
      a0 = __builtin_amdgcn_mfma_f32_16x16x16bf16_1k(__builtin_bit_cast(s16x4, v0), __builtin_bit_cast(s16x4, bb), a0, 0, 0, 0);
      a1 = __builtin_amdgcn_mfma_f32_16x16x16bf16_1k(__builtin_bit_cast(s16x4, v1), __builtin_bit_cast(s16x4, bb), a1, 0, 0, 0);
      __builtin_amdgcn_sched_barrier(0);
      if (LOAD.value) {
        pace(tnext);
        r[s] = __builtin_amdgcn_raw_buffer_load_b128(rv, voff, ldoff((blk + 1) * RING + s), AUX);
        __builtin_amdgcn_sched_barrier(0);
      }
    }
  };
  for (int blk = 0; blk < NBLK - 1; ++blk) {
    if (blk == NBLK / 2) { h0a0 = a0; h0a1 = a1; l0 = l; a0 = f32x4{0.f, 0.f, 0.f, 0.f}; a1 = a0; l = 0.f; m = m1; }
    vblock(blk, std::true_type{});
  }
  const unsigned ts3 = tnext;
  vblock(NBLK - 1, std::false_type{});
  const unsigned tk1 = clk();
  const float L0 = all_sum(l0), L1 = all_sum(l);
  float* mg = &sm[w][0][0];
#pragma unroll
  for (int rho = 0; rho < 4; ++rho) {
    const int d0 = 8 * (4 * g + rho) + j4;
    mg[r4 * 132 + d0] = h0a0[rho]; mg[r4 * 132 + d0 + 4] = h0a1[rho];
    mg[(4 + r4) * 132 + d0] = a0[rho]; mg[(4 + r4) * 132 + d0 + 4] = a1[rho];
  }
  if (g == 0 && j4 == 0) { mg[r4 * 132 + 128] = m0; mg[r4 * 132 + 129] = L0; mg[(4 + r4) * 132 + 128] = m1; mg[(4 + r4) * 132 + 129] = L1; }
  __syncthreads();
  {
    const int row = tid >> 5, d0 = (tid & 31) * 4;
    float mw[4], M = -INFINITY;
#pragma unroll
    for (int i = 0; i < 4; ++i) { mw[i] = (&sm[i][0][0])[row * 132 + 128]; M = fmaxf(M, mw[i]); }
    float Ls = 0.f; f32x4 o = {0.f, 0.f, 0.f, 0.f};
#pragma unroll
    for (int i = 0; i < 4; ++i) {
      const float f = __builtin_amdgcn_exp2f(mw[i] - M);
      const float* rec = &sm[i][0][0] + row * 132;
      Ls = fmaf(f, rec[129], Ls);
      o += f * *reinterpret_cast<const f32x4*>(rec + d0);
    }
    const float inv = 1.f / Ls;
    const u32x2 ov = {cvtpk(o[0] * inv, o[1] * inv), cvtpk(o[2] * inv, o[3] * inv)};
    *reinterpret_cast<u32x2*>(O + ((size_t)b * 8 + row) * D + d0) = ov;
  }
  asm volatile("s_waitcnt vmcnt(0)" ::: "memory");
  const unsigned tk2 = clk();
  if (tid == 0) printf("WG %d t0 %u K %d V %d end %d\n", b, tk0 >> 8, (int)(ts2 - tk0) >> 8, (int)(ts3 - tk0) >> 8, (int)(tk2 - tk0) >> 8);
}

extern "C" void kernel_launch(void* const* d_in, const int* in_sizes, int n_in, void* d_out, int out_size, void* d_ws, size_t ws_size,
                              hipStream_t stream) {
  fprintf(stderr, "PTRS q %p k %p v %p o %p ws %p\n", d_in[0], d_in[1], d_in[2], d_out, d_ws);
  attn_kernel<<<NBH / 2, 256, 0, stream>>>((const __hip_bfloat16*)d_in[0], (const __hip_bfloat16*)d_in[1], (const __hip_bfloat16*)d_in[2],
                                           (__hip_bfloat16*)d_out
, (unsigned*)d_ws
);
}
```

```cpp
#include <hip/hip_runtime.h>
#include <hip/hip_bf16.h>
#include <stdint.h>

constexpr int NWG = 256;
constexpr unsigned PART = 131072, MX = PART + 2 * 9856, LDS_BYTES = MX + 128;

__global__ void __launch_bounds__(256) attn_kernel(const __hip_bfloat16* __restrict__ Q, const __hip_bfloat16* __restrict__ K,
                                                   const __hip_bfloat16* __restrict__ V, __hip_bfloat16* __restrict__ O) {
  __shared__ __attribute__((aligned(16))) char sm[LDS_BYTES];
  const unsigned lds = (unsigned)(uintptr_t)(__attribute__((address_space(3))) char*)sm;
  const unsigned b = blockIdx.x, tid = threadIdx.x;
  const char* pc; unsigned co, cd;
  asm volatile("s_getpc_b64 %[pc]\n.Lattn_pc:\nv_and_b32 %[co], 63, %[tid]\nv_lshlrev_b32 %[co], 7, %[co]\n"
               "v_min_u32 %[co], .Lattn_end-.Lattn_pc, %[co]\n"
               "global_load_dword %[cd], %[co], %[pc]\n.Lattn_np:"
               : [pc] "=&s"(pc), [co] "=&v"(co), [cd] "=&v"(cd) : [tid] "v"(tid), [b] "s"(b) : "memory", "scc");
  __builtin_amdgcn_sched_barrier(0);
  const unsigned lane = tid & 63, w = __builtin_amdgcn_readfirstlane(tid >> 6);
  const unsigned g = lane >> 4, r4 = (lane >> 2) & 3, j4 = lane & 3, id = b * 4 + w;
  constexpr unsigned L0 = 885760, L1 = 40704;
  const unsigned pb = b;
  unsigned so = ((((id >> 0) & 1u) * 72192u) ^ (((id >> 1) & 1u) * 820736u)) | (pb << 21);
  unsigned qo = pb * 2048 + r4 * 256 + g * 64 + j4 * 16;
  unsigned ko = ((r4 & 1) * L0 ^ (r4 >> 1) * L1) + g * 64 + j4 * 16;
  unsigned oo = pb * 2048 + w * 256 + lane * 4;
  asm volatile("" : "+v"(qo), "+v"(ko), "+v"(oo), "+s"(so));
  __builtin_amdgcn_sched_barrier(0);
  asm volatile(
      "s_mov_b64 s[40:41], %[k]\n"
      "s_and_b32 s41, s41, 0xffff\n"
      "s_mov_b32 s42, 0x20000000\n"
      "s_mov_b32 s43, 0x20000\n"
      "v_xor_b32 v15, %[so], %[ko]\n"
      "buffer_load_dwordx4 v[124:127], v15, s[40:43], 0 offen nt\n"
      "s_memrealtime s[46:47]\n"
      "global_load_dwordx4 v[20:23], %[qo], %[q]\n"
      "global_load_dwordx4 v[24:27], %[qo], %[q] offset:1024\n"
      "s_mov_b32 s36, m0\n"
      "s_mov_b32 s49, %[so]\n"
      "s_mov_b32 s65, 927488\n"
      "s_mov_b32 s79, 160\n"
      "s_mov_b32 s80, 44\n"
      "s_mov_b32 s81, 45\n"
      "s_mov_b32 s82, 45\n"
      "s_mov_b32 s83, 45\n"
      "s_mov_b32 s85, 212\n"
      "s_waitcnt lgkmcnt(0)\n"
      "s_lshl_b32 s48, s46, 4\n"
      "s_sub_u32 s48, 0, s48\n"
      "s_sub_u32 s48, s48, 204\n"
      "s_mov_b32 s58, s48\n"
      "s_xor_b32 s50, s49, s65\n"
      "v_xor_b32 v15, s50, %[ko]\n"
      ".Lp1_%=:\n"
      "s_memrealtime s[46:47]\n"
      "s_waitcnt lgkmcnt(0)\n"
      "s_lshl4_add_u32 s45, s46, s48\n"
      "s_cmp_lt_i32 s45, 0\n"
      "s_cbranch_scc1 .Lp1_%=\n"
      "buffer_load_dwordx4 v[128:131], v15, s[40:43], 0 offen nt\n"
      "s_max_i32 s45, s45, s79\n"
      "s_add_u32 s45, s45, s81\n"
      "s_sub_u32 s48, s48, s45\n"
      "s_sub_u32 s58, s58, s85\n"
      "s_min_i32 s48, s48, s58\n"
      "s_lshr_b32 s94, %[b], 3\n"
      "s_mul_i32 s94, s94, 25\n"
      "s_lshr_b32 s94, s94, 5\n"
      "s_add_u32 s94, s94, 5\n"
      "s_cmp_eq_u32 %[w], 0\n"
      "s_cselect_b32 s94, s94, 32\n"
      "s_mov_b32 s66, 962560\n"
      "v_bfe_u32 v84, %[tid], 4, 2\n"
      "v_bfe_u32 v85, %[tid], 2, 2\n"
      "v_and_b32 v86, 3, %[tid]\n"
      "v_and_b32 v87, 15, %[tid]\n"
      "s_lshl_b32 s50, %[w], 15\n"
      "s_add_u32 s50, s50, %[lds]\n"
      "v_and_b32 v88, 63, %[tid]\n"
      "v_lshl_add_u32 v113, v88, 2, s50\n"
      "s_xor_b32 s50, s49, s66\n"
      "v_xor_b32 v15, s50, %[ko]\n"
      ".Lp2_%=:\n"
      "s_memrealtime s[46:47]\n"
      "s_waitcnt lgkmcnt(0)\n"
      "s_lshl4_add_u32 s45, s46, s48\n"
      "s_cmp_lt_i32 s45, 0\n"
      "s_cbranch_scc1 .Lp2_%=\n"
      "buffer_load_dwordx4 v[132:135], v15, s[40:43], 0 offen nt\n"
      "s_max_i32 s45, s45, s79\n"
      "s_add_u32 s45, s45, s82\n"
      "s_sub_u32 s48, s48, s45\n"
      "s_sub_u32 s58, s58, s85\n"
      "s_min_i32 s48, s48, s58\n"
      "s_mov_b32 s67, 38656\n"
      "s_mov_b32 s68, 796928\n"
      "s_mov_b32 s69, 134656\n"
      "s_mov_b32 s70, 170240\n"
      "s_mov_b32 s71, 835072\n"
      "s_mov_b32 s72, 635392\n"
      "s_mov_b32 s73, 634112\n"
      "v_and_b32 v88, 1, v84\n"
      "v_mul_u32_u24 v88, 885760, v88\n"
      "v_lshrrev_b32 v89, 1, v84\n"
      "v_mul_u32_u24 v89, 40704, v89\n"
      "v_xor_b32 v88, v88, v89\n"
      "v_lshl_add_u32 v112, v87, 4, v88\n"
      "s_xor_b32 s50, s49, s67\n"
      "v_xor_b32 v15, s50, %[ko]\n"
      ".Lp3_%=:\n"
      "s_memrealtime s[46:47]\n"
      "s_waitcnt lgkmcnt(0)\n"
      "s_lshl4_add_u32 s45, s46, s48\n"
      "s_cmp_lt_i32 s45, 0\n"
      "s_cbranch_scc1 .Lp3_%=\n"
      "buffer_load_dwordx4 v[136:139], v15, s[40:43], 0 offen nt\n"
      "s_max_i32 s45, s45, s79\n"
      "s_add_u32 s45, s45, s83\n"
      "s_sub_u32 s48, s48, s45\n"
      "s_sub_u32 s58, s58, s85\n"
      "s_min_i32 s48, s48, s58\n"
      "s_mov_b32 s74, 1022976\n"
      "s_mov_b32 s75, 773888\n"
      "s_mov_b32 s76, 997120\n"
      "s_mov_b32 s77, 1910016\n"
      "s_mov_b32 s78, 1910016\n"
      "s_mov_b32 s88, 6\n"
      "s_mov_b64 s[38:39], %[v]\n"
      "s_mul_i32 s50, %[w], 2464\n"
      "s_add_u32 s50, s50, %[lds]\n"
      "s_add_u32 s50, s50, 131072\n"
      "v_mul_u32_u24 v88, 608, v85\n"
      "v_add_u32 v88, s50, v88\n"
      "v_add_u32 v116, 576, v88\n"
      "v_mul_u32_u24 v89, 144, v84\n"
      "v_add_u32 v88, v88, v89\n"
      "v_lshl_add_u32 v114, v86, 2, v88\n"
      "s_xor_b32 s50, s49, s68\n"
      "v_xor_b32 v15, s50, %[ko]\n"
      ".Lp4_%=:\n"
      "s_memrealtime s[46:47]\n"
      "s_waitcnt lgkmcnt(0)\n"
      "s_lshl4_add_u32 s45, s46, s48\n"
      "s_cmp_lt_i32 s45, 0\n"
      "s_cbranch_scc1 .Lp4_%=\n"
      "buffer_load_dwordx4 v[140:143], v15, s[40:43], 0 offen nt\n"
      "s_max_i32 s45, s45, s79\n"
      "s_add_u32 s45, s45, s80\n"
      "s_sub_u32 s48, s48, s45\n"
      "s_sub_u32 s58, s58, s85\n"
      "s_min_i32 s48, s48, s58\n"
      "s_and_b32 s39, s39, 0xffff\n"
      "s_mov_b32 s52, 0x22222222\n"
      "s_mov_b32 s53, s52\n"
      "s_mov_b32 s54, 0x44444444\n"
      "s_mov_b32 s55, s54\n"
      "s_mov_b32 s56, 0x88888888\n"
      "s_mov_b32 s57, s56\n"
      "s_mul_i32 s50, %[w], 608\n"
      "s_add_u32 s50, s50, %[lds]\n"
      "s_add_u32 s50, s50, 131072\n"
      "v_add_u32 v88, s50, v89\n"
      "v_lshl_add_u32 v115, v87, 3, v88\n"
      "s_add_u32 s50, s50, 576\n"
      "v_mov_b32 v117, s50\n"
      "s_xor_b32 s50, s49, s69\n"
      "v_xor_b32 v15, s50, %[ko]\n"
      ".Lp5_%=:\n"
      "s_memrealtime s[46:47]\n"
      "s_waitcnt lgkmcnt(0)\n"
      "s_lshl4_add_u32 s45, s46, s48\n"
      "s_cmp_lt_i32 s45, 0\n"
      "s_cbranch_scc1 .Lp5_%=\n"
      "buffer_load_dwordx4 v[144:147], v15, s[40:43], 0 offen nt\n"
      "s_max_i32 s45, s45, s79\n"
      "s_add_u32 s45, s45, s81\n"
      "s_sub_u32 s48, s48, s45\n"
      "s_sub_u32 s58, s58, s85\n"
      "s_min_i32 s48, s48, s58\n"
      "s_mov_b32 s62, 0x3e0293ee\n"
      "s_mov_b32 s63, 0xbe0293ee\n"
      "s_mov_b32 s51, 0\n"
      "s_mov_b32 s37, 4\n"
      "v_mov_b32 v11, 0xff800000\n"
      "v_mov_b32 v72, 0\n"
      "v_mov_b32 v64, 0\n"
      "s_lshl2_add_u32 s50, %[w], %[lds]\n"
      "s_add_u32 s50, s50, 150784\n"
      "v_lshl_add_u32 v118, v85, 4, s50\n"
      "s_lshl4_add_u32 s50, %[w], %[lds]\n"
      "s_add_u32 s50, s50, 150784\n"
      "v_mov_b32 v119, s50\n"
      "s_xor_b32 s50, s49, s70\n"
      "v_xor_b32 v15, s50, %[ko]\n"
      ".Lp6_%=:\n"
      "s_memrealtime s[46:47]\n"
      "s_waitcnt lgkmcnt(0)\n"
      "s_lshl4_add_u32 s45, s46, s48\n"
      "s_cmp_lt_i32 s45, 0\n"
      "s_cbranch_scc1 .Lp6_%=\n"
      "buffer_load_dwordx4 v[148:151], v15, s[40:43], 0 offen nt\n"
      "s_max_i32 s45, s45, s79\n"
      "s_add_u32 s45, s45, s82\n"
      "s_sub_u32 s48, s48, s45\n"
      "s_sub_u32 s58, s58, s85\n"
      "s_min_i32 s48, s48, s58\n"
      "v_mov_b32 v65, 0\n"
      "v_mov_b32 v66, 0\n"
      "v_mov_b32 v67, 0\n"
      "v_mov_b32 v68, 0\n"
      "v_mov_b32 v69, 0\n"
      "v_mov_b32 v70, 0\n"
      "v_mov_b32 v71, 0\n"
      "v_lshlrev_b32 v88, 4, v86\n"
      "v_mov_b32 v90, 0xffff\n"
      "v_mov_b32 v91, 0\n"
      "v_lshlrev_b64 v[120:121], v88, v[90:91]\n"
      "s_xor_b32 s50, s49, s71\n"
      "v_xor_b32 v15, s50, %[ko]\n"
      ".Lp7_%=:\n"
      "s_memrealtime s[46:47]\n"
      "s_waitcnt lgkmcnt(0)\n"
      "s_lshl4_add_u32 s45, s46, s48\n"
      "s_cmp_lt_i32 s45, 0\n"
      "s_cbranch_scc1 .Lp7_%=\n"
      "buffer_load_dwordx4 v[152:155], v15, s[40:43], 0 offen nt\n"
      "s_max_i32 s45, s45, s79\n"
      "s_add_u32 s45, s45, s83\n"
      "s_sub_u32 s48, s48, s45\n"
      "s_sub_u32 s58, s58, s85\n"
      "s_min_i32 s48, s48, s58\n"
      "s_mov_b32 s93, 3520\n"
      "s_add_u32 s96, s85, 3520\n"
      "s_mov_b32 s92, 2147483647\n"
      "s_mov_b32 s60, 0\n"
      "s_mov_b32 s89, 46\n"
      "v_mov_b32 v10, v113\n"
      "s_sub_u32 s58, s58, s88\n"
      "s_mov_b32 s51, 1\n"
      "s_xor_b32 s49, s49, s72\n"
      "s_mov_b32 s50, s49\n"
      "v_xor_b32 v15, s50, %[ko]\n"
      ".Lp8_%=:\n"
      "s_memrealtime s[46:47]\n"
      "s_waitcnt lgkmcnt(0)\n"
      "s_lshl4_add_u32 s45, s46, s48\n"
      "s_cmp_lt_i32 s45, 0\n"
      "s_cbranch_scc1 .Lp8_%=\n"
      "buffer_load_dwordx4 v[32:35], v15, s[40:43], 0 offen nt\n"
      "s_max_i32 s45, s45, s79\n"
      "s_add_u32 s45, s45, s80\n"
      "s_sub_u32 s48, s48, s45\n"
      "s_sub_u32 s58, s58, s85\n"
      "s_min_i32 s48, s48, s58\n"
      "s_xor_b32 s50, s49, s65\n"
      "v_xor_b32 v15, s50, %[ko]\n"
      ".Lp9_%=:\n"
      "s_memrealtime s[46:47]\n"
      "s_waitcnt lgkmcnt(0)\n"
      "s_lshl4_add_u32 s45, s46, s48\n"
      "s_cmp_lt_i32 s45, 0\n"
      "s_cbranch_scc1 .Lp9_%=\n"
      "buffer_load_dwordx4 v[36:39], v15, s[40:43], 0 offen nt\n"
      "s_max_i32 s45, s45, s79\n"
      "s_add_u32 s45, s45, s81\n"
      "s_sub_u32 s48, s48, s45\n"
      "s_sub_u32 s58, s58, s85\n"
      "s_min_i32 s48, s48, s58\n"
      "s_xor_b32 s50, s49, s66\n"
      "v_xor_b32 v15, s50, %[ko]\n"
      ".Lp10_%=:\n"
      "s_memrealtime s[46:47]\n"
      "s_waitcnt lgkmcnt(0)\n"
      "s_lshl4_add_u32 s45, s46, s48\n"
      "s_cmp_lt_i32 s45, 0\n"
      "s_cbranch_scc1 .Lp10_%=\n"
      "buffer_load_dwordx4 v[40:43], v15, s[40:43], 0 offen nt\n"
      "s_max_i32 s45, s45, s79\n"
      "s_add_u32 s45, s45, s82\n"
      "s_sub_u32 s48, s48, s45\n"
      "s_sub_u32 s58, s58, s85\n"
      "s_min_i32 s48, s48, s58\n"
      "s_xor_b32 s50, s49, s67\n"
      "v_xor_b32 v15, s50, %[ko]\n"
      ".Lp11_%=:\n"
      "s_memrealtime s[46:47]\n"
      "s_waitcnt lgkmcnt(0)\n"
      "s_lshl4_add_u32 s45, s46, s48\n"
      "s_cmp_lt_i32 s45, 0\n"
      "s_cbranch_scc1 .Lp11_%=\n"
      "buffer_load_dwordx4 v[44:47], v15, s[40:43], 0 offen nt\n"
      "s_max_i32 s45, s45, s79\n"
      "s_add_u32 s45, s45, s83\n"
      "s_sub_u32 s48, s48, s45\n"
      "s_sub_u32 s58, s58, s85\n"
      "s_min_i32 s48, s48, s58\n"
      "s_xor_b32 s50, s49, s68\n"
      "v_xor_b32 v15, s50, %[ko]\n"
      ".Lp12_%=:\n"
      "s_memrealtime s[46:47]\n"
      "s_waitcnt lgkmcnt(0)\n"
      "s_lshl4_add_u32 s45, s46, s48\n"
      "s_cmp_lt_i32 s45, 0\n"
      "s_cbranch_scc1 .Lp12_%=\n"
      "buffer_load_dwordx4 v[48:51], v15, s[40:43], 0 offen nt\n"
      "s_max_i32 s45, s45, s79\n"
      "s_add_u32 s45, s45, s80\n"
      "s_sub_u32 s48, s48, s45\n"
      "s_sub_u32 s58, s58, s85\n"
      "s_min_i32 s48, s48, s58\n"
      "s_xor_b32 s50, s49, s69\n"
      "v_xor_b32 v15, s50, %[ko]\n"
      ".Lp13_%=:\n"
      "s_memrealtime s[46:47]\n"
      "s_waitcnt lgkmcnt(0)\n"
      "s_lshl4_add_u32 s45, s46, s48\n"
      "s_cmp_lt_i32 s45, 0\n"
      "s_cbranch_scc1 .Lp13_%=\n"
      "buffer_load_dwordx4 v[52:55], v15, s[40:43], 0 offen nt\n"
      "s_max_i32 s45, s45, s79\n"
      "s_add_u32 s45, s45, s81\n"
      "s_sub_u32 s48, s48, s45\n"
      "s_sub_u32 s58, s58, s85\n"
      "s_min_i32 s48, s48, s58\n"
      "s_xor_b32 s50, s49, s70\n"
      "v_xor_b32 v15, s50, %[ko]\n"
      ".Lp14_%=:\n"
      "s_memrealtime s[46:47]\n"
      "s_waitcnt lgkmcnt(0)\n"
      "s_lshl4_add_u32 s45, s46, s48\n"
      "s_cmp_lt_i32 s45, 0\n"
      "s_cbranch_scc1 .Lp14_%=\n"
      "buffer_load_dwordx4 v[56:59], v15, s[40:43], 0 offen nt\n"
      "s_max_i32 s45, s45, s79\n"
      "s_add_u32 s45, s45, s82\n"
      "s_sub_u32 s48, s48, s45\n"
      "s_sub_u32 s58, s58, s85\n"
      "s_min_i32 s48, s48, s58\n"
      "s_xor_b32 s50, s49, s71\n"
      "v_xor_b32 v15, s50, %[ko]\n"
      ".Lp15_%=:\n"
      "s_memrealtime s[46:47]\n"
      "s_waitcnt lgkmcnt(0)\n"
      "s_lshl4_add_u32 s45, s46, s48\n"
      "s_cmp_lt_i32 s45, 0\n"
      "s_cbranch_scc1 .Lp15_%=\n"
      "buffer_load_dwordx4 v[60:63], v15, s[40:43], 0 offen nt\n"
      "s_max_i32 s45, s45, s79\n"
      "s_add_u32 s45, s45, s83\n"
      "s_sub_u32 s48, s48, s45\n"
      "s_sub_u32 s58, s58, s85\n"
      "s_min_i32 s48, s48, s58\n"
      "s_waitcnt vmcnt(15)\n"
      "v_mfma_f32_16x16x32_bf16 v[16:19], v[124:127], v[20:23], 0\n"
      "s_waitcnt vmcnt(14)\n"
      "v_mfma_f32_16x16x32_bf16 v[100:103], v[128:131], v[20:23], 0\n"
      "s_nop 5\n"
      "v_mov_b32_dpp v13, v16 quad_perm:[0,0,0,0] row_mask:0xf bank_mask:0xf\n"
      "v_mov_b32_dpp v14, v18 quad_perm:[2,2,2,2] row_mask:0xf bank_mask:0xf\n"
      "s_nop 0\n"
      "v_add_f32_dpp v13, v17, v13 quad_perm:[1,1,1,1] row_mask:0xf bank_mask:0xf\n"
      "v_add_f32_dpp v14, v19, v14 quad_perm:[3,3,3,3] row_mask:0xf bank_mask:0xf\n"
      "v_add_f32 v12, v13, v14\n"
      "s_waitcnt vmcnt(13)\n"
      "v_mfma_f32_16x16x32_bf16 v[16:19], v[132:135], v[20:23], 0\n"
      "v_mov_b32_dpp v13, v100 quad_perm:[0,0,0,0] row_mask:0xf bank_mask:0xf\n"
      "v_mov_b32_dpp v14, v102 quad_perm:[2,2,2,2] row_mask:0xf bank_mask:0xf\n"
      "s_nop 0\n"
      "v_add_f32_dpp v13, v101, v13 quad_perm:[1,1,1,1] row_mask:0xf bank_mask:0xf\n"
      "v_add_f32_dpp v14, v103, v14 quad_perm:[3,3,3,3] row_mask:0xf bank_mask:0xf\n"
      "v_add_f32 v13, v13, v14\n"
      "v_cndmask_b32_e64 v12, v12, v13, s[52:53]\n"
      "s_waitcnt vmcnt(12)\n"
      "v_mfma_f32_16x16x32_bf16 v[100:103], v[136:139], v[20:23], 0\n"
      "v_mov_b32_dpp v13, v16 quad_perm:[0,0,0,0] row_mask:0xf bank_mask:0xf\n"
      "v_mov_b32_dpp v14, v18 quad_perm:[2,2,2,2] row_mask:0xf bank_mask:0xf\n"
      "s_nop 0\n"
      "v_add_f32_dpp v13, v17, v13 quad_perm:[1,1,1,1] row_mask:0xf bank_mask:0xf\n"
      "v_add_f32_dpp v14, v19, v14 quad_perm:[3,3,3,3] row_mask:0xf bank_mask:0xf\n"
      "v_add_f32 v13, v13, v14\n"
      "v_cndmask_b32_e64 v12, v12, v13, s[54:55]\n"
      "s_waitcnt vmcnt(11)\n"
      "v_mfma_f32_16x16x32_bf16 v[16:19], v[140:143], v[20:23], 0\n"
      "v_mov_b32_dpp v13, v100 quad_perm:[0,0,0,0] row_mask:0xf bank_mask:0xf\n"
      "v_mov_b32_dpp v14, v102 quad_perm:[2,2,2,2] row_mask:0xf bank_mask:0xf\n"
      "s_nop 0\n"
      "v_add_f32_dpp v13, v101, v13 quad_perm:[1,1,1,1] row_mask:0xf bank_mask:0xf\n"
      "v_add_f32_dpp v14, v103, v14 quad_perm:[3,3,3,3] row_mask:0xf bank_mask:0xf\n"
      "v_add_f32 v13, v13, v14\n"
      "v_cndmask_b32_e64 v12, v12, v13, s[56:57]\n"
      "v_max_f32 v11, v11, v12\n"
      "ds_write_b32 v10, v12 offset:0\n"
      "s_waitcnt vmcnt(10)\n"
      "v_mfma_f32_16x16x32_bf16 v[100:103], v[144:147], v[20:23], 0\n"
      "v_mov_b32_dpp v13, v16 quad_perm:[0,0,0,0] row_mask:0xf bank_mask:0xf\n"
      "v_mov_b32_dpp v14, v18 quad_perm:[2,2,2,2] row_mask:0xf bank_mask:0xf\n"
      "s_nop 0\n"
      "v_add_f32_dpp v13, v17, v13 quad_perm:[1,1,1,1] row_mask:0xf bank_mask:0xf\n"
      "v_add_f32_dpp v14, v19, v14 quad_perm:[3,3,3,3] row_mask:0xf bank_mask:0xf\n"
      "v_add_f32 v12, v13, v14\n"
      "s_waitcnt vmcnt(9)\n"
      "v_mfma_f32_16x16x32_bf16 v[16:19], v[148:151], v[20:23], 0\n"
      "v_mov_b32_dpp v13, v100 quad_perm:[0,0,0,0] row_mask:0xf bank_mask:0xf\n"
      "v_mov_b32_dpp v14, v102 quad_perm:[2,2,2,2] row_mask:0xf bank_mask:0xf\n"
      "s_nop 0\n"
      "v_add_f32_dpp v13, v101, v13 quad_perm:[1,1,1,1] row_mask:0xf bank_mask:0xf\n"
      "v_add_f32_dpp v14, v103, v14 quad_perm:[3,3,3,3] row_mask:0xf bank_mask:0xf\n"
      "v_add_f32 v13, v13, v14\n"
      "v_cndmask_b32_e64 v12, v12, v13, s[52:53]\n"
      "s_waitcnt vmcnt(8)\n"
      "v_mfma_f32_16x16x32_bf16 v[100:103], v[152:155], v[20:23], 0\n"
      "v_mov_b32_dpp v13, v16 quad_perm:[0,0,0,0] row_mask:0xf bank_mask:0xf\n"
      "v_mov_b32_dpp v14, v18 quad_perm:[2,2,2,2] row_mask:0xf bank_mask:0xf\n"
      "s_nop 0\n"
      "v_add_f32_dpp v13, v17, v13 quad_perm:[1,1,1,1] row_mask:0xf bank_mask:0xf\n"
      "v_add_f32_dpp v14, v19, v14 quad_perm:[3,3,3,3] row_mask:0xf bank_mask:0xf\n"
      "v_add_f32 v13, v13, v14\n"
      "v_cndmask_b32_e64 v12, v12, v13, s[54:55]\n"
      "s_nop 1\n"
      "v_mov_b32_dpp v13, v100 quad_perm:[0,0,0,0] row_mask:0xf bank_mask:0xf\n"
      "v_mov_b32_dpp v14, v102 quad_perm:[2,2,2,2] row_mask:0xf bank_mask:0xf\n"
      "s_nop 0\n"
      "v_add_f32_dpp v13, v101, v13 quad_perm:[1,1,1,1] row_mask:0xf bank_mask:0xf\n"
      "v_add_f32_dpp v14, v103, v14 quad_perm:[3,3,3,3] row_mask:0xf bank_mask:0xf\n"
      "v_add_f32 v13, v13, v14\n"
      "v_cndmask_b32_e64 v12, v12, v13, s[56:57]\n"
      "v_max_f32 v11, v11, v12\n"
      "ds_write_b32 v10, v12 offset:256\n"
      "v_add_u32 v10, 512, v10\n"
      ".LK_%=:\n"
      "s_sub_u32 s50, s58, s48\n"
      "s_lshr_b32 s50, s50, 6\n"
      "s_sub_u32 s50, s89, s50\n"
      "s_max_i32 s50, s50, 12\n"
      "s_mul_i32 s50, s50, 0x20100\n"
      "s_add_u32 s61, s50, 0xa8180000\n"
      "s_sub_u32 s58, s58, s88\n"
      "s_add_u32 s51, s51, 1\n"
      "s_ff1_i32_b32 s50, s51\n"
      "s_mov_b32 m0, s50\n"
      "s_nop 0\n"
      "s_movrels_b32 s50, s72\n"
      "s_xor_b32 s49, s49, s50\n"
      "s_mov_b32 s50, s49\n"
      "s_waitcnt vmcnt(7)\n"
      "v_mfma_f32_16x16x32_bf16 v[16:19], v[32:35], v[20:23], 0\n"
      "v_xor_b32 v15, s50, %[ko]\n"
      ".Lp16_%=:\n"
      "s_memrealtime s[46:47]\n"
      "s_waitcnt lgkmcnt(0)\n"
      "s_lshl4_add_u32 s45, s46, s48\n"
      "s_cmp_lt_i32 s45, 0\n"
      "s_cbranch_scc1 .Lp16_%=\n"
      "s_getreg_b32 s44, hwreg(HW_REG_IB_STS, 0, 4)\n"
      "buffer_load_dwordx4 v[32:35], v15, s[40:43], 0 offen nt\n"
      "s_cmp_gt_i32 s45, s92\n"
      "s_cselect_b32 s47, s93, s79\n"
      "s_cselect_b32 s59, s96, s85\n"
      "s_max_i32 s45, s45, s47\n"
      "s_add_u32 s45, s45, s80\n"
      "s_lshl3_add_u32 s44, s44, 0x80000\n"
      "s_bfe_u64 s[46:47], s[60:61], s44\n"
      "s_add_u32 s45, s45, s46\n"
      "s_sub_u32 s48, s48, s45\n"
      "s_sub_u32 s58, s58, s59\n"
      "s_min_i32 s48, s48, s58\n"
      "v_mov_b32_dpp v13, v16 quad_perm:[0,0,0,0] row_mask:0xf bank_mask:0xf\n"
      "v_mov_b32_dpp v14, v18 quad_perm:[2,2,2,2] row_mask:0xf bank_mask:0xf\n"
      "s_nop 0\n"
      "v_add_f32_dpp v13, v17, v13 quad_perm:[1,1,1,1] row_mask:0xf bank_mask:0xf\n"
      "v_add_f32_dpp v14, v19, v14 quad_perm:[3,3,3,3] row_mask:0xf bank_mask:0xf\n"
      "v_add_f32 v12, v13, v14\n"
      "s_xor_b32 s50, s49, s65\n"
      "s_waitcnt vmcnt(7)\n"
      "v_mfma_f32_16x16x32_bf16 v[16:19], v[36:39], v[20:23], 0\n"
      "v_xor_b32 v15, s50, %[ko]\n"
      ".Lp17_%=:\n"
      "s_memrealtime s[46:47]\n"
      "s_waitcnt lgkmcnt(0)\n"
      "s_lshl4_add_u32 s45, s46, s48\n"
      "s_cmp_lt_i32 s45, 0\n"
      "s_cbranch_scc1 .Lp17_%=\n"
      "s_getreg_b32 s44, hwreg(HW_REG_IB_STS, 0, 4)\n"
      "buffer_load_dwordx4 v[36:39], v15, s[40:43], 0 offen nt\n"
      "s_cmp_gt_i32 s45, s92\n"
      "s_cselect_b32 s47, s93, s79\n"
      "s_cselect_b32 s59, s96, s85\n"
      "s_max_i32 s45, s45, s47\n"
      "s_add_u32 s45, s45, s81\n"
      "s_lshl3_add_u32 s44, s44, 0x80000\n"
      "s_bfe_u64 s[46:47], s[60:61], s44\n"
      "s_add_u32 s45, s45, s46\n"
      "s_sub_u32 s48, s48, s45\n"
      "s_sub_u32 s58, s58, s59\n"
      "s_min_i32 s48, s48, s58\n"
      "v_mov_b32_dpp v13, v16 quad_perm:[0,0,0,0] row_mask:0xf bank_mask:0xf\n"
      "v_mov_b32_dpp v14, v18 quad_perm:[2,2,2,2] row_mask:0xf bank_mask:0xf\n"
      "s_nop 0\n"
      "v_add_f32_dpp v13, v17, v13 quad_perm:[1,1,1,1] row_mask:0xf bank_mask:0xf\n"
      "v_add_f32_dpp v14, v19, v14 quad_perm:[3,3,3,3] row_mask:0xf bank_mask:0xf\n"
      "v_add_f32 v13, v13, v14\n"
      "v_cndmask_b32_e64 v12, v12, v13, s[52:53]\n"
      "s_xor_b32 s50, s49, s66\n"
      "s_waitcnt vmcnt(7)\n"
      "v_mfma_f32_16x16x32_bf16 v[16:19], v[40:43], v[20:23], 0\n"
      "v_xor_b32 v15, s50, %[ko]\n"
      ".Lp18_%=:\n"
      "s_memrealtime s[46:47]\n"
      "s_waitcnt lgkmcnt(0)\n"
      "s_lshl4_add_u32 s45, s46, s48\n"
      "s_cmp_lt_i32 s45, 0\n"
      "s_cbranch_scc1 .Lp18_%=\n"
      "s_getreg_b32 s44, hwreg(HW_REG_IB_STS, 0, 4)\n"
      "buffer_load_dwordx4 v[40:43], v15, s[40:43], 0 offen nt\n"
      "s_cmp_gt_i32 s45, s92\n"
      "s_cselect_b32 s47, s93, s79\n"
      "s_cselect_b32 s59, s96, s85\n"
      "s_max_i32 s45, s45, s47\n"
      "s_add_u32 s45, s45, s82\n"
      "s_lshl3_add_u32 s44, s44, 0x80000\n"
      "s_bfe_u64 s[46:47], s[60:61], s44\n"
      "s_add_u32 s45, s45, s46\n"
      "s_sub_u32 s48, s48, s45\n"
      "s_sub_u32 s58, s58, s59\n"
      "s_min_i32 s48, s48, s58\n"
      "v_mov_b32_dpp v13, v16 quad_perm:[0,0,0,0] row_mask:0xf bank_mask:0xf\n"
      "v_mov_b32_dpp v14, v18 quad_perm:[2,2,2,2] row_mask:0xf bank_mask:0xf\n"
      "s_nop 0\n"
      "v_add_f32_dpp v13, v17, v13 quad_perm:[1,1,1,1] row_mask:0xf bank_mask:0xf\n"
      "v_add_f32_dpp v14, v19, v14 quad_perm:[3,3,3,3] row_mask:0xf bank_mask:0xf\n"
      "v_add_f32 v13, v13, v14\n"
      "v_cndmask_b32_e64 v12, v12, v13, s[54:55]\n"
      "s_xor_b32 s50, s49, s67\n"
      "s_waitcnt vmcnt(7)\n"
      "v_mfma_f32_16x16x32_bf16 v[16:19], v[44:47], v[20:23], 0\n"
      "v_xor_b32 v15, s50, %[ko]\n"
      ".Lp19_%=:\n"
      "s_memrealtime s[46:47]\n"
      "s_waitcnt lgkmcnt(0)\n"
      "s_lshl4_add_u32 s45, s46, s48\n"
      "s_cmp_lt_i32 s45, 0\n"
      "s_cbranch_scc1 .Lp19_%=\n"
      "s_getreg_b32 s44, hwreg(HW_REG_IB_STS, 0, 4)\n"
      "buffer_load_dwordx4 v[44:47], v15, s[40:43], 0 offen nt\n"
      "s_cmp_gt_i32 s45, s92\n"
      "s_cselect_b32 s47, s93, s79\n"
      "s_cselect_b32 s59, s96, s85\n"
      "s_max_i32 s45, s45, s47\n"
      "s_add_u32 s45, s45, s83\n"
      "s_lshl3_add_u32 s44, s44, 0x80000\n"
      "s_bfe_u64 s[46:47], s[60:61], s44\n"
      "s_add_u32 s45, s45, s46\n"
      "s_sub_u32 s48, s48, s45\n"
      "s_sub_u32 s58, s58, s59\n"
      "s_min_i32 s48, s48, s58\n"
      "v_mov_b32_dpp v13, v16 quad_perm:[0,0,0,0] row_mask:0xf bank_mask:0xf\n"
      "v_mov_b32_dpp v14, v18 quad_perm:[2,2,2,2] row_mask:0xf bank_mask:0xf\n"
      "s_nop 0\n"
      "v_add_f32_dpp v13, v17, v13 quad_perm:[1,1,1,1] row_mask:0xf bank_mask:0xf\n"
      "v_add_f32_dpp v14, v19, v14 quad_perm:[3,3,3,3] row_mask:0xf bank_mask:0xf\n"
      "v_add_f32 v13, v13, v14\n"
      "v_cndmask_b32_e64 v12, v12, v13, s[56:57]\n"
      "v_max_f32 v11, v11, v12\n"
      "ds_write_b32 v10, v12 offset:0\n"
      "s_xor_b32 s50, s49, s68\n"
      "s_waitcnt vmcnt(7)\n"
      "v_mfma_f32_16x16x32_bf16 v[16:19], v[48:51], v[20:23], 0\n"
      "v_xor_b32 v15, s50, %[ko]\n"
      ".Lp20_%=:\n"
      "s_memrealtime s[46:47]\n"
      "s_waitcnt lgkmcnt(0)\n"
      "s_lshl4_add_u32 s45, s46, s48\n"
      "s_cmp_lt_i32 s45, 0\n"
      "s_cbranch_scc1 .Lp20_%=\n"
      "s_getreg_b32 s44, hwreg(HW_REG_IB_STS, 0, 4)\n"
      "buffer_load_dwordx4 v[48:51], v15, s[40:43], 0 offen nt\n"
      "s_cmp_gt_i32 s45, s92\n"
      "s_cselect_b32 s47, s93, s79\n"
      "s_cselect_b32 s59, s96, s85\n"
      "s_max_i32 s45, s45, s47\n"
      "s_add_u32 s45, s45, s80\n"
      "s_lshl3_add_u32 s44, s44, 0x80000\n"
      "s_bfe_u64 s[46:47], s[60:61], s44\n"
      "s_add_u32 s45, s45, s46\n"
      "s_sub_u32 s48, s48, s45\n"
      "s_sub_u32 s58, s58, s59\n"
      "s_min_i32 s48, s48, s58\n"
      "v_mov_b32_dpp v13, v16 quad_perm:[0,0,0,0] row_mask:0xf bank_mask:0xf\n"
      "v_mov_b32_dpp v14, v18 quad_perm:[2,2,2,2] row_mask:0xf bank_mask:0xf\n"
      "s_nop 0\n"
      "v_add_f32_dpp v13, v17, v13 quad_perm:[1,1,1,1] row_mask:0xf bank_mask:0xf\n"
      "v_add_f32_dpp v14, v19, v14 quad_perm:[3,3,3,3] row_mask:0xf bank_mask:0xf\n"
      "v_add_f32 v12, v13, v14\n"
      "s_xor_b32 s50, s49, s69\n"
      "s_waitcnt vmcnt(7)\n"
      "v_mfma_f32_16x16x32_bf16 v[16:19], v[52:55], v[20:23], 0\n"
      "v_xor_b32 v15, s50, %[ko]\n"
      ".Lp21_%=:\n"
      "s_memrealtime s[46:47]\n"
      "s_waitcnt lgkmcnt(0)\n"
      "s_lshl4_add_u32 s45, s46, s48\n"
      "s_cmp_lt_i32 s45, 0\n"
      "s_cbranch_scc1 .Lp21_%=\n"
      "s_getreg_b32 s44, hwreg(HW_REG_IB_STS, 0, 4)\n"
      "buffer_load_dwordx4 v[52:55], v15, s[40:43], 0 offen nt\n"
      "s_cmp_gt_i32 s45, s92\n"
      "s_cselect_b32 s47, s93, s79\n"
      "s_cselect_b32 s59, s96, s85\n"
      "s_max_i32 s45, s45, s47\n"
      "s_add_u32 s45, s45, s81\n"
      "s_lshl3_add_u32 s44, s44, 0x80000\n"
      "s_bfe_u64 s[46:47], s[60:61], s44\n"
      "s_add_u32 s45, s45, s46\n"
      "s_sub_u32 s48, s48, s45\n"
      "s_sub_u32 s58, s58, s59\n"
      "s_min_i32 s48, s48, s58\n"
      "v_mov_b32_dpp v13, v16 quad_perm:[0,0,0,0] row_mask:0xf bank_mask:0xf\n"
      "v_mov_b32_dpp v14, v18 quad_perm:[2,2,2,2] row_mask:0xf bank_mask:0xf\n"
      "s_nop 0\n"
      "v_add_f32_dpp v13, v17, v13 quad_perm:[1,1,1,1] row_mask:0xf bank_mask:0xf\n"
      "v_add_f32_dpp v14, v19, v14 quad_perm:[3,3,3,3] row_mask:0xf bank_mask:0xf\n"
      "v_add_f32 v13, v13, v14\n"
      "v_cndmask_b32_e64 v12, v12, v13, s[52:53]\n"
      "s_xor_b32 s50, s49, s70\n"
      "s_waitcnt vmcnt(7)\n"
      "v_mfma_f32_16x16x32_bf16 v[16:19], v[56:59], v[20:23], 0\n"
      "v_xor_b32 v15, s50, %[ko]\n"
      ".Lp22_%=:\n"
      "s_memrealtime s[46:47]\n"
      "s_waitcnt lgkmcnt(0)\n"
      "s_lshl4_add_u32 s45, s46, s48\n"
      "s_cmp_lt_i32 s45, 0\n"
      "s_cbranch_scc1 .Lp22_%=\n"
      "s_getreg_b32 s44, hwreg(HW_REG_IB_STS, 0, 4)\n"
      "buffer_load_dwordx4 v[56:59], v15, s[40:43], 0 offen nt\n"
      "s_cmp_gt_i32 s45, s92\n"
      "s_cselect_b32 s47, s93, s79\n"
      "s_cselect_b32 s59, s96, s85\n"
      "s_max_i32 s45, s45, s47\n"
      "s_add_u32 s45, s45, s82\n"
      "s_lshl3_add_u32 s44, s44, 0x80000\n"
      "s_bfe_u64 s[46:47], s[60:61], s44\n"
      "s_add_u32 s45, s45, s46\n"
      "s_sub_u32 s48, s48, s45\n"
      "s_sub_u32 s58, s58, s59\n"
      "s_min_i32 s48, s48, s58\n"
      "v_mov_b32_dpp v13, v16 quad_perm:[0,0,0,0] row_mask:0xf bank_mask:0xf\n"
      "v_mov_b32_dpp v14, v18 quad_perm:[2,2,2,2] row_mask:0xf bank_mask:0xf\n"
      "s_nop 0\n"
      "v_add_f32_dpp v13, v17, v13 quad_perm:[1,1,1,1] row_mask:0xf bank_mask:0xf\n"
      "v_add_f32_dpp v14, v19, v14 quad_perm:[3,3,3,3] row_mask:0xf bank_mask:0xf\n"
      "v_add_f32 v13, v13, v14\n"
      "v_cndmask_b32_e64 v12, v12, v13, s[54:55]\n"
      "s_xor_b32 s50, s49, s71\n"
      "s_waitcnt vmcnt(7)\n"
      "v_mfma_f32_16x16x32_bf16 v[16:19], v[60:63], v[20:23], 0\n"
      "v_xor_b32 v15, s50, %[ko]\n"
      ".Lp23_%=:\n"
      "s_memrealtime s[46:47]\n"
      "s_waitcnt lgkmcnt(0)\n"
      "s_lshl4_add_u32 s45, s46, s48\n"
      "s_cmp_lt_i32 s45, 0\n"
      "s_cbranch_scc1 .Lp23_%=\n"
      "s_getreg_b32 s44, hwreg(HW_REG_IB_STS, 0, 4)\n"
      "buffer_load_dwordx4 v[60:63], v15, s[40:43], 0 offen nt\n"
      "s_cmp_gt_i32 s45, s92\n"
      "s_cselect_b32 s47, s93, s79\n"
      "s_cselect_b32 s59, s96, s85\n"
      "s_max_i32 s45, s45, s47\n"
      "s_add_u32 s45, s45, s83\n"
      "s_lshl3_add_u32 s44, s44, 0x80000\n"
      "s_bfe_u64 s[46:47], s[60:61], s44\n"
      "s_add_u32 s45, s45, s46\n"
      "s_sub_u32 s48, s48, s45\n"
      "s_sub_u32 s58, s58, s59\n"
      "s_min_i32 s48, s48, s58\n"
      "v_mov_b32_dpp v13, v16 quad_perm:[0,0,0,0] row_mask:0xf bank_mask:0xf\n"
      "v_mov_b32_dpp v14, v18 quad_perm:[2,2,2,2] row_mask:0xf bank_mask:0xf\n"
      "s_nop 0\n"
      "v_add_f32_dpp v13, v17, v13 quad_perm:[1,1,1,1] row_mask:0xf bank_mask:0xf\n"
      "v_add_f32_dpp v14, v19, v14 quad_perm:[3,3,3,3] row_mask:0xf bank_mask:0xf\n"
      "v_add_f32 v13, v13, v14\n"
      "v_cndmask_b32_e64 v12, v12, v13, s[56:57]\n"
      "v_max_f32 v11, v11, v12\n"
      "ds_write_b32 v10, v12 offset:256\n"
      "v_add_u32 v10, 512, v10\n"
      "s_cmp_lg_u32 s51, s37\n"
      "s_cbranch_scc1 .LK_%=\n"
      "s_cmp_eq_u32 s51, 4\n"
      "s_cselect_b32 s92, 1760, s92\n"
      "s_cselect_b32 s37, s94, s37\n"
      "s_cbranch_scc1 .LK_%=\n"
      "s_cmp_lt_u32 s51, 32\n"
      "s_cbranch_scc1 .LKT_%=\n"
      "s_cmp_eq_u32 s51, 63\n"
      "s_cbranch_scc1 .LK63_%=\n"
      "s_nop 1\n"
      "v_max_f32_dpp v13, v11, v11 quad_perm:[1,0,3,2] row_mask:0xf bank_mask:0xf\n"
      "s_nop 1\n"
      "v_max_f32_dpp v13, v13, v13 quad_perm:[2,3,0,1] row_mask:0xf bank_mask:0xf\n"
      "v_mov_b32 v14, v13\n"
      "s_nop 1\n"
      "v_permlane16_swap_b32 v13, v14\n"
      "v_max_f32 v13, v13, v14\n"
      "v_mov_b32 v14, v13\n"
      "s_nop 1\n"
      "v_permlane32_swap_b32 v13, v14\n"
      "v_max_f32 v13, v13, v14\n"
      "s_cmp_eq_u32 s51, 64\n"
      "s_cbranch_scc1 .LK64_%=\n"
      "v_mul_f32 v73, s63, v13\n"
      "ds_read_b32 v78, v113\n"
      "ds_write_b32 v118, v73\n"
      "v_mov_b32 v20, v24\n"
      "v_mov_b32 v21, v25\n"
      "v_mov_b32 v22, v26\n"
      "v_mov_b32 v23, v27\n"
      "v_mov_b32 v11, 0xff800000\n"
      "s_mov_b32 s37, 63\n"
      "s_branch .LK_%=\n"
      ".LKT_%=:\n"
      "v_mov_b32 v28, %[so]\n"
      "global_load_dword v28, v28, %[v] nt\n"
      "global_load_dword v31, %[oo], %[o]\n"
      "s_mov_b32 s37, 32\n"
      "s_branch .LK_%=\n"
      ".LK63_%=:\n"
      "s_mov_b64 s[40:41], s[38:39]\n"
      "v_mov_b32 %[ko], v112\n"
      "global_load_dword %[cd], %[co], %[pc]\n"
      "s_mov_b32 s37, 64\n"
      "s_branch .LK_%=\n"
      ".LK64_%=:\n"
      "v_mul_f32 v79, s63, v13\n"
      "v_mov_b32 v10, v113\n"
      "s_mov_b32 s51, 0\n"
      "s_mov_b32 s37, 32\n"
      "ds_write_b32 v118, v79 offset:64\n"
      "s_sub_u32 s89, s89, 6\n"
      ".LV_%=:\n"
      "s_sub_u32 s50, s58, s48\n"
      "s_lshr_b32 s50, s50, 6\n"
      "s_sub_u32 s50, s89, s50\n"
      "s_max_i32 s50, s50, 12\n"
      "s_mul_i32 s50, s50, 0x20100\n"
      "s_add_u32 s61, s50, 0xa8180000\n"
      "s_sub_u32 s58, s58, s88\n"
      "s_add_u32 s51, s51, 1\n"
      "s_ff1_i32_b32 s50, s51\n"
      "s_mov_b32 m0, s50\n"
      "s_nop 0\n"
      "s_movrels_b32 s50, s72\n"
      "s_xor_b32 s49, s49, s50\n"
      "v_fma_f32 v74, v78, s62, v73\n"
      "v_exp_f32 v74, v74\n"
      "ds_read_b32 v78, v10 offset:256\n"
      "v_cvt_pk_bf16_f32 v75, v74, v74\n"
      "v_add_f32 v72, v72, v74\n"
      "s_nop 0\n"
      "v_and_b32_dpp v76, v75, v120 quad_perm:[0,0,0,0] row_mask:0xf bank_mask:0xf\n"
      "v_and_b32_dpp v77, v75, v121 quad_perm:[0,0,0,0] row_mask:0xf bank_mask:0xf\n"
      "s_waitcnt vmcnt(7)\n"
      "s_mov_b32 s50, s49\n"
      "v_mfma_f32_16x16x16_bf16 v[64:67], v[32:33], v[76:77], v[64:67]\n"
      "v_mfma_f32_16x16x16_bf16 v[68:71], v[34:35], v[76:77], v[68:71]\n"
      "v_xor_b32 v15, s50, %[ko]\n"
      ".Lp25_%=:\n"
      "s_memrealtime s[46:47]\n"
      "s_waitcnt lgkmcnt(0)\n"
      "s_lshl4_add_u32 s45, s46, s48\n"
      "s_cmp_lt_i32 s45, 0\n"
      "s_cbranch_scc1 .Lp25_%=\n"
      "s_getreg_b32 s44, hwreg(HW_REG_IB_STS, 0, 4)\n"
      "buffer_load_dwordx4 v[32:35], v15, s[40:43], 0 offen nt\n"
      "s_cmp_gt_i32 s45, s92\n"
      "s_cselect_b32 s47, s93, s79\n"
      "s_cselect_b32 s59, s96, s85\n"
      "s_max_i32 s45, s45, s47\n"
      "s_add_u32 s45, s45, s80\n"
      "s_lshl3_add_u32 s44, s44, 0x80000\n"
      "s_bfe_u64 s[46:47], s[60:61], s44\n"
      "s_add_u32 s45, s45, s46\n"
      "s_sub_u32 s48, s48, s45\n"
      "s_sub_u32 s58, s58, s59\n"
      "s_min_i32 s48, s48, s58\n"
      "v_and_b32_dpp v76, v75, v120 quad_perm:[1,1,1,1] row_mask:0xf bank_mask:0xf\n"
      "v_and_b32_dpp v77, v75, v121 quad_perm:[1,1,1,1] row_mask:0xf bank_mask:0xf\n"
      "s_waitcnt vmcnt(7)\n"
      "s_xor_b32 s50, s49, s65\n"
      "v_mfma_f32_16x16x16_bf16 v[64:67], v[36:37], v[76:77], v[64:67]\n"
      "v_mfma_f32_16x16x16_bf16 v[68:71], v[38:39], v[76:77], v[68:71]\n"
      "v_xor_b32 v15, s50, %[ko]\n"
      ".Lp26_%=:\n"
      "s_memrealtime s[46:47]\n"
      "s_waitcnt lgkmcnt(0)\n"
      "s_lshl4_add_u32 s45, s46, s48\n"
      "s_cmp_lt_i32 s45, 0\n"
      "s_cbranch_scc1 .Lp26_%=\n"
      "s_getreg_b32 s44, hwreg(HW_REG_IB_STS, 0, 4)\n"
      "buffer_load_dwordx4 v[36:39], v15, s[40:43], 0 offen nt\n"
      "s_cmp_gt_i32 s45, s92\n"
      "s_cselect_b32 s47, s93, s79\n"
      "s_cselect_b32 s59, s96, s85\n"
      "s_max_i32 s45, s45, s47\n"
      "s_add_u32 s45, s45, s81\n"
      "s_lshl3_add_u32 s44, s44, 0x80000\n"
      "s_bfe_u64 s[46:47], s[60:61], s44\n"
      "s_add_u32 s45, s45, s46\n"
      "s_sub_u32 s48, s48, s45\n"
      "s_sub_u32 s58, s58, s59\n"
      "s_min_i32 s48, s48, s58\n"
      "v_and_b32_dpp v76, v75, v120 quad_perm:[2,2,2,2] row_mask:0xf bank_mask:0xf\n"
      "v_and_b32_dpp v77, v75, v121 quad_perm:[2,2,2,2] row_mask:0xf bank_mask:0xf\n"
      "s_waitcnt vmcnt(7)\n"
      "s_xor_b32 s50, s49, s66\n"
      "v_mfma_f32_16x16x16_bf16 v[64:67], v[40:41], v[76:77], v[64:67]\n"
      "v_mfma_f32_16x16x16_bf16 v[68:71], v[42:43], v[76:77], v[68:71]\n"
      "v_xor_b32 v15, s50, %[ko]\n"
      ".Lp27_%=:\n"
      "s_memrealtime s[46:47]\n"
      "s_waitcnt lgkmcnt(0)\n"
      "s_lshl4_add_u32 s45, s46, s48\n"
      "s_cmp_lt_i32 s45, 0\n"
      "s_cbranch_scc1 .Lp27_%=\n"
      "s_getreg_b32 s44, hwreg(HW_REG_IB_STS, 0, 4)\n"
      "buffer_load_dwordx4 v[40:43], v15, s[40:43], 0 offen nt\n"
      "s_cmp_gt_i32 s45, s92\n"
      "s_cselect_b32 s47, s93, s79\n"
      "s_cselect_b32 s59, s96, s85\n"
      "s_max_i32 s45, s45, s47\n"
      "s_add_u32 s45, s45, s82\n"
      "s_lshl3_add_u32 s44, s44, 0x80000\n"
      "s_bfe_u64 s[46:47], s[60:61], s44\n"
      "s_add_u32 s45, s45, s46\n"
      "s_sub_u32 s48, s48, s45\n"
      "s_sub_u32 s58, s58, s59\n"
      "s_min_i32 s48, s48, s58\n"
      "v_and_b32_dpp v76, v75, v120 quad_perm:[3,3,3,3] row_mask:0xf bank_mask:0xf\n"
      "v_and_b32_dpp v77, v75, v121 quad_perm:[3,3,3,3] row_mask:0xf bank_mask:0xf\n"
      "s_waitcnt vmcnt(7)\n"
      "s_xor_b32 s50, s49, s67\n"
      "v_mfma_f32_16x16x16_bf16 v[64:67], v[44:45], v[76:77], v[64:67]\n"
      "v_mfma_f32_16x16x16_bf16 v[68:71], v[46:47], v[76:77], v[68:71]\n"
      "v_xor_b32 v15, s50, %[ko]\n"
      ".Lp28_%=:\n"
      "s_memrealtime s[46:47]\n"
      "s_waitcnt lgkmcnt(0)\n"
      "s_lshl4_add_u32 s45, s46, s48\n"
      "s_cmp_lt_i32 s45, 0\n"
      "s_cbranch_scc1 .Lp28_%=\n"
      "s_getreg_b32 s44, hwreg(HW_REG_IB_STS, 0, 4)\n"
      "buffer_load_dwordx4 v[44:47], v15, s[40:43], 0 offen nt\n"
      "s_cmp_gt_i32 s45, s92\n"
      "s_cselect_b32 s47, s93, s79\n"
      "s_cselect_b32 s59, s96, s85\n"
      "s_max_i32 s45, s45, s47\n"
      "s_add_u32 s45, s45, s83\n"
      "s_lshl3_add_u32 s44, s44, 0x80000\n"
      "s_bfe_u64 s[46:47], s[60:61], s44\n"
      "s_add_u32 s45, s45, s46\n"
      "s_sub_u32 s48, s48, s45\n"
      "s_sub_u32 s58, s58, s59\n"
      "s_min_i32 s48, s48, s58\n"
      "v_fma_f32 v74, v78, s62, v73\n"
      "v_exp_f32 v74, v74\n"
      "ds_read_b32 v78, v10 offset:512\n"
      "v_cvt_pk_bf16_f32 v75, v74, v74\n"
      "v_add_f32 v72, v72, v74\n"
      "s_nop 0\n"
      "v_and_b32_dpp v76, v75, v120 quad_perm:[0,0,0,0] row_mask:0xf bank_mask:0xf\n"
      "v_and_b32_dpp v77, v75, v121 quad_perm:[0,0,0,0] row_mask:0xf bank_mask:0xf\n"
      "s_waitcnt vmcnt(7)\n"
      "s_xor_b32 s50, s49, s68\n"
      "v_mfma_f32_16x16x16_bf16 v[64:67], v[48:49], v[76:77], v[64:67]\n"
      "v_mfma_f32_16x16x16_bf16 v[68:71], v[50:51], v[76:77], v[68:71]\n"
      "v_xor_b32 v15, s50, %[ko]\n"
      ".Lp29_%=:\n"
      "s_memrealtime s[46:47]\n"
      "s_waitcnt lgkmcnt(0)\n"
      "s_lshl4_add_u32 s45, s46, s48\n"
      "s_cmp_lt_i32 s45, 0\n"
      "s_cbranch_scc1 .Lp29_%=\n"
      "s_getreg_b32 s44, hwreg(HW_REG_IB_STS, 0, 4)\n"
      "buffer_load_dwordx4 v[48:51], v15, s[40:43], 0 offen nt\n"
      "s_cmp_gt_i32 s45, s92\n"
      "s_cselect_b32 s47, s93, s79\n"
      "s_cselect_b32 s59, s96, s85\n"
      "s_max_i32 s45, s45, s47\n"
      "s_add_u32 s45, s45, s80\n"
      "s_lshl3_add_u32 s44, s44, 0x80000\n"
      "s_bfe_u64 s[46:47], s[60:61], s44\n"
      "s_add_u32 s45, s45, s46\n"
      "s_sub_u32 s48, s48, s45\n"
      "s_sub_u32 s58, s58, s59\n"
      "s_min_i32 s48, s48, s58\n"
      "v_and_b32_dpp v76, v75, v120 quad_perm:[1,1,1,1] row_mask:0xf bank_mask:0xf\n"
      "v_and_b32_dpp v77, v75, v121 quad_perm:[1,1,1,1] row_mask:0xf bank_mask:0xf\n"
      "s_waitcnt vmcnt(7)\n"
      "s_xor_b32 s50, s49, s69\n"
      "v_mfma_f32_16x16x16_bf16 v[64:67], v[52:53], v[76:77], v[64:67]\n"
      "v_mfma_f32_16x16x16_bf16 v[68:71], v[54:55], v[76:77], v[68:71]\n"
      "v_xor_b32 v15, s50, %[ko]\n"
      ".Lp30_%=:\n"
      "s_memrealtime s[46:47]\n"
      "s_waitcnt lgkmcnt(0)\n"
      "s_lshl4_add_u32 s45, s46, s48\n"
      "s_cmp_lt_i32 s45, 0\n"
      "s_cbranch_scc1 .Lp30_%=\n"
      "s_getreg_b32 s44, hwreg(HW_REG_IB_STS, 0, 4)\n"
      "buffer_load_dwordx4 v[52:55], v15, s[40:43], 0 offen nt\n"
      "s_cmp_gt_i32 s45, s92\n"
      "s_cselect_b32 s47, s93, s79\n"
      "s_cselect_b32 s59, s96, s85\n"
      "s_max_i32 s45, s45, s47\n"
      "s_add_u32 s45, s45, s81\n"
      "s_lshl3_add_u32 s44, s44, 0x80000\n"
      "s_bfe_u64 s[46:47], s[60:61], s44\n"
      "s_add_u32 s45, s45, s46\n"
      "s_sub_u32 s48, s48, s45\n"
      "s_sub_u32 s58, s58, s59\n"
      "s_min_i32 s48, s48, s58\n"
      "v_and_b32_dpp v76, v75, v120 quad_perm:[2,2,2,2] row_mask:0xf bank_mask:0xf\n"
      "v_and_b32_dpp v77, v75, v121 quad_perm:[2,2,2,2] row_mask:0xf bank_mask:0xf\n"
      "s_waitcnt vmcnt(7)\n"
      "s_xor_b32 s50, s49, s70\n"
      "v_mfma_f32_16x16x16_bf16 v[64:67], v[56:57], v[76:77], v[64:67]\n"
      "v_mfma_f32_16x16x16_bf16 v[68:71], v[58:59], v[76:77], v[68:71]\n"
      "v_xor_b32 v15, s50, %[ko]\n"
      ".Lp31_%=:\n"
      "s_memrealtime s[46:47]\n"
      "s_waitcnt lgkmcnt(0)\n"
      "s_lshl4_add_u32 s45, s46, s48\n"
      "s_cmp_lt_i32 s45, 0\n"
      "s_cbranch_scc1 .Lp31_%=\n"
      "s_getreg_b32 s44, hwreg(HW_REG_IB_STS, 0, 4)\n"
      "buffer_load_dwordx4 v[56:59], v15, s[40:43], 0 offen nt\n"
      "s_cmp_gt_i32 s45, s92\n"
      "s_cselect_b32 s47, s93, s79\n"
      "s_cselect_b32 s59, s96, s85\n"
      "s_max_i32 s45, s45, s47\n"
      "s_add_u32 s45, s45, s82\n"
      "s_lshl3_add_u32 s44, s44, 0x80000\n"
      "s_bfe_u64 s[46:47], s[60:61], s44\n"
      "s_add_u32 s45, s45, s46\n"
      "s_sub_u32 s48, s48, s45\n"
      "s_sub_u32 s58, s58, s59\n"
      "s_min_i32 s48, s48, s58\n"
      "v_and_b32_dpp v76, v75, v120 quad_perm:[3,3,3,3] row_mask:0xf bank_mask:0xf\n"
      "v_and_b32_dpp v77, v75, v121 quad_perm:[3,3,3,3] row_mask:0xf bank_mask:0xf\n"
      "s_waitcnt vmcnt(7)\n"
      "s_xor_b32 s50, s49, s71\n"
      "v_mfma_f32_16x16x16_bf16 v[64:67], v[60:61], v[76:77], v[64:67]\n"
      "v_mfma_f32_16x16x16_bf16 v[68:71], v[62:63], v[76:77], v[68:71]\n"
      "v_xor_b32 v15, s50, %[ko]\n"
      ".Lp32_%=:\n"
      "s_memrealtime s[46:47]\n"
      "s_waitcnt lgkmcnt(0)\n"
      "s_lshl4_add_u32 s45, s46, s48\n"
      "s_cmp_lt_i32 s45, 0\n"
      "s_cbranch_scc1 .Lp32_%=\n"
      "s_getreg_b32 s44, hwreg(HW_REG_IB_STS, 0, 4)\n"
      "buffer_load_dwordx4 v[60:63], v15, s[40:43], 0 offen nt\n"
      "s_cmp_gt_i32 s45, s92\n"
      "s_cselect_b32 s47, s93, s79\n"
      "s_cselect_b32 s59, s96, s85\n"
      "s_max_i32 s45, s45, s47\n"
      "s_add_u32 s45, s45, s83\n"
      "s_lshl3_add_u32 s44, s44, 0x80000\n"
      "s_bfe_u64 s[46:47], s[60:61], s44\n"
      "s_add_u32 s45, s45, s46\n"
      "s_sub_u32 s48, s48, s45\n"
      "s_sub_u32 s58, s58, s59\n"
      "s_min_i32 s48, s48, s58\n"
      "v_add_u32 v10, 512, v10\n"
      "s_cmp_lg_u32 s51, s37\n"
      "s_cbranch_scc1 .LV_%=\n"
      "s_cmp_eq_u32 s51, 63\n"
      "s_cbranch_scc1 .LT_%=\n"
      "s_cmp_eq_u32 s51, 32\n"
      "s_cbranch_scc1 .LV32_%=\n"
      "s_cmp_eq_u32 s51, 48\n"
      "s_cbranch_scc1 .LV48_%=\n"
      "s_cmp_eq_u32 s51, 49\n"
      "s_cbranch_scc1 .LV49_%=\n"
      "v_min3_f32 v13, v104, v105, v106\n"
      "v_min_f32 v13, v13, v107\n"
      "v_sub_f32 v20, v13, v104\n"
      "v_sub_f32 v21, v13, v105\n"
      "v_sub_f32 v22, v13, v106\n"
      "v_sub_f32 v23, v13, v107\n"
      "v_exp_f32 v20, v20\n"
      "v_exp_f32 v21, v21\n"
      "v_exp_f32 v22, v22\n"
      "v_exp_f32 v23, v23\n"
      "s_nop 0\n"
      "s_sub_u32 s89, s89, 8\n"
      "s_mov_b32 s37, 63\n"
      "s_branch .LV_%=\n"
      ".LV48_%=:\n"
      "global_load_dword %[cd], %[co], %[pc]\n"
      "s_barrier\n"
      "ds_read_b128 v[100:103], v119 offset:0\n"
      "ds_read_b32 v92, v117 offset:0\n"
      "ds_read_b32 v93, v117 offset:2464\n"
      "ds_read_b32 v94, v117 offset:4928\n"
      "ds_read_b32 v95, v117 offset:7392\n"
      "ds_read_b64 v[84:85], v115 offset:0\n"
      "ds_read_b64 v[86:87], v115 offset:2464\n"
      "ds_read_b64 v[88:89], v115 offset:4928\n"
      "ds_read_b64 v[90:91], v115 offset:7392\n"
      "ds_read_b128 v[104:107], v119 offset:64\n"
      "s_mov_b32 s37, 49\n"
      "s_branch .LV_%=\n"
      ".LV49_%=:\n"
      "v_min3_f32 v13, v100, v101, v102\n"
      "v_min_f32 v13, v13, v103\n"
      "v_sub_f32 v16, v13, v100\n"
      "v_sub_f32 v17, v13, v101\n"
      "v_sub_f32 v18, v13, v102\n"
      "v_sub_f32 v19, v13, v103\n"
      "v_exp_f32 v16, v16\n"
      "v_exp_f32 v17, v17\n"
      "v_exp_f32 v18, v18\n"
      "v_exp_f32 v19, v19\n"
      "s_nop 0\n"
      "v_mul_f32 v13, v16, v92\n"
      "v_fmac_f32 v13, v17, v93\n"
      "v_fmac_f32 v13, v18, v94\n"
      "v_fmac_f32 v13, v19, v95\n"
      "v_rcp_f32 v13, v13\n"
      "s_nop 0\n"
      "v_mul_f32 v16, v16, v13\n"
      "v_mul_f32 v17, v17, v13\n"
      "v_mul_f32 v18, v18, v13\n"
      "v_mul_f32 v19, v19, v13\n"
      "v_mul_f32 v13, v16, v84\n"
      "v_mul_f32 v14, v16, v85\n"
      "v_fmac_f32 v13, v17, v86\n"
      "v_fmac_f32 v14, v17, v87\n"
      "v_fmac_f32 v13, v18, v88\n"
      "v_fmac_f32 v14, v18, v89\n"
      "v_fmac_f32 v13, v19, v90\n"
      "v_fmac_f32 v14, v19, v91\n"
      "v_cvt_pk_bf16_f32 v82, v13, v14\n"
      "s_mov_b32 s37, 50\n"
      "s_branch .LV_%=\n"
      ".LV32_%=:\n"
      "ds_write2_b32 v114, v64, v68 offset0:0 offset1:4\n"
      "ds_write2_b32 v114, v65, v69 offset0:8 offset1:12\n"
      "ds_write2_b32 v114, v66, v70 offset0:16 offset1:20\n"
      "ds_write2_b32 v114, v67, v71 offset0:24 offset1:28\n"
      "s_nop 1\n"
      "v_add_f32_dpp v13, v72, v72 quad_perm:[1,0,3,2] row_mask:0xf bank_mask:0xf\n"
      "s_nop 1\n"
      "v_add_f32_dpp v13, v13, v13 quad_perm:[2,3,0,1] row_mask:0xf bank_mask:0xf\n"
      "v_mov_b32 v14, v13\n"
      "s_nop 1\n"
      "v_permlane16_swap_b32 v13, v14\n"
      "v_add_f32 v13, v13, v14\n"
      "v_mov_b32 v14, v13\n"
      "s_nop 1\n"
      "v_permlane32_swap_b32 v13, v14\n"
      "v_add_f32 v13, v13, v14\n"
      "ds_write_b32 v116, v13\n"
      "v_mov_b32 v64, 0\n"
      "v_mov_b32 v65, 0\n"
      "v_mov_b32 v66, 0\n"
      "v_mov_b32 v67, 0\n"
      "v_mov_b32 v68, 0\n"
      "v_mov_b32 v69, 0\n"
      "v_mov_b32 v70, 0\n"
      "v_mov_b32 v71, 0\n"
      "v_mov_b32 v72, 0\n"
      "v_mov_b32 v73, v79\n"
      "s_sub_u32 s89, s89, 8\n"
      "v_add_u32 v114, 9856, v114\n"
      "s_mov_b32 s37, 48\n"
      "s_branch .LV_%=\n"
      ".LT_%=:\n"
      "ds_read_b32 v80, v10 offset:256\n"
      "v_fma_f32 v74, v78, s62, v73\n"
      "v_exp_f32 v74, v74\n"
      "s_nop 0\n"
      "v_cvt_pk_bf16_f32 v75, v74, v74\n"
      "v_add_f32 v72, v72, v74\n"
      "s_waitcnt lgkmcnt(0)\n"
      "v_fma_f32 v74, v80, s62, v73\n"
      "v_exp_f32 v74, v74\n"
      "s_nop 0\n"
      "v_cvt_pk_bf16_f32 v81, v74, v74\n"
      "v_add_f32 v72, v72, v74\n"
      "s_nop 1\n"
      "v_add_f32_dpp v13, v72, v72 quad_perm:[1,0,3,2] row_mask:0xf bank_mask:0xf\n"
      "s_nop 1\n"
      "v_add_f32_dpp v13, v13, v13 quad_perm:[2,3,0,1] row_mask:0xf bank_mask:0xf\n"
      "v_mov_b32 v14, v13\n"
      "s_nop 1\n"
      "v_permlane16_swap_b32 v13, v14\n"
      "v_add_f32 v13, v13, v14\n"
      "v_mov_b32 v14, v13\n"
      "s_nop 1\n"
      "v_permlane32_swap_b32 v13, v14\n"
      "v_add_f32 v13, v13, v14\n"
      "ds_write_b32 v116, v13 offset:9856\n"
      "s_waitcnt lgkmcnt(0)\n"
      "s_barrier\n"
      "ds_read_b32 v108, v117 offset:9856\n"
      "ds_read_b32 v109, v117 offset:12320\n"
      "ds_read_b32 v110, v117 offset:14784\n"
      "ds_read_b32 v111, v117 offset:17248\n"
      "v_and_b32_dpp v76, v75, v120 quad_perm:[0,0,0,0] row_mask:0xf bank_mask:0xf\n"
      "v_and_b32_dpp v77, v75, v121 quad_perm:[0,0,0,0] row_mask:0xf bank_mask:0xf\n"
      "s_waitcnt vmcnt(7)\n"
      "s_nop 0\n"
      "v_mfma_f32_16x16x16_bf16 v[64:67], v[32:33], v[76:77], v[64:67]\n"
      "v_mfma_f32_16x16x16_bf16 v[68:71], v[34:35], v[76:77], v[68:71]\n"
      "v_and_b32_dpp v76, v75, v120 quad_perm:[1,1,1,1] row_mask:0xf bank_mask:0xf\n"
      "v_and_b32_dpp v77, v75, v121 quad_perm:[1,1,1,1] row_mask:0xf bank_mask:0xf\n"
      "s_waitcnt vmcnt(6)\n"
      "s_nop 0\n"
      "v_mfma_f32_16x16x16_bf16 v[64:67], v[36:37], v[76:77], v[64:67]\n"
      "v_mfma_f32_16x16x16_bf16 v[68:71], v[38:39], v[76:77], v[68:71]\n"
      "v_and_b32_dpp v76, v75, v120 quad_perm:[2,2,2,2] row_mask:0xf bank_mask:0xf\n"
      "v_and_b32_dpp v77, v75, v121 quad_perm:[2,2,2,2] row_mask:0xf bank_mask:0xf\n"
      "s_waitcnt vmcnt(5)\n"
      "s_nop 0\n"
      "v_mfma_f32_16x16x16_bf16 v[64:67], v[40:41], v[76:77], v[64:67]\n"
      "v_mfma_f32_16x16x16_bf16 v[68:71], v[42:43], v[76:77], v[68:71]\n"
      "v_and_b32_dpp v76, v75, v120 quad_perm:[3,3,3,3] row_mask:0xf bank_mask:0xf\n"
      "v_and_b32_dpp v77, v75, v121 quad_perm:[3,3,3,3] row_mask:0xf bank_mask:0xf\n"
      "s_waitcnt vmcnt(4)\n"
      "s_nop 0\n"
      "v_mfma_f32_16x16x16_bf16 v[64:67], v[44:45], v[76:77], v[64:67]\n"
      "v_mfma_f32_16x16x16_bf16 v[68:71], v[46:47], v[76:77], v[68:71]\n"
      "s_waitcnt lgkmcnt(0)\n"
      "v_mul_f32 v13, v20, v108\n"
      "v_fmac_f32 v13, v21, v109\n"
      "v_fmac_f32 v13, v22, v110\n"
      "v_fmac_f32 v13, v23, v111\n"
      "v_rcp_f32 v13, v13\n"
      "s_nop 0\n"
      "v_mul_f32 v20, v20, v13\n"
      "v_mul_f32 v21, v21, v13\n"
      "v_mul_f32 v22, v22, v13\n"
      "v_mul_f32 v23, v23, v13\n"
      "v_and_b32_dpp v76, v81, v120 quad_perm:[0,0,0,0] row_mask:0xf bank_mask:0xf\n"
      "v_and_b32_dpp v77, v81, v121 quad_perm:[0,0,0,0] row_mask:0xf bank_mask:0xf\n"
      "s_waitcnt vmcnt(3)\n"
      "s_nop 0\n"
      "v_mfma_f32_16x16x16_bf16 v[64:67], v[48:49], v[76:77], v[64:67]\n"
      "v_mfma_f32_16x16x16_bf16 v[68:71], v[50:51], v[76:77], v[68:71]\n"
      "v_and_b32_dpp v76, v81, v120 quad_perm:[1,1,1,1] row_mask:0xf bank_mask:0xf\n"
      "v_and_b32_dpp v77, v81, v121 quad_perm:[1,1,1,1] row_mask:0xf bank_mask:0xf\n"
      "s_waitcnt vmcnt(2)\n"
      "s_nop 0\n"
      "v_mfma_f32_16x16x16_bf16 v[64:67], v[52:53], v[76:77], v[64:67]\n"
      "v_mfma_f32_16x16x16_bf16 v[68:71], v[54:55], v[76:77], v[68:71]\n"
      "v_and_b32_dpp v76, v81, v120 quad_perm:[2,2,2,2] row_mask:0xf bank_mask:0xf\n"
      "v_and_b32_dpp v77, v81, v121 quad_perm:[2,2,2,2] row_mask:0xf bank_mask:0xf\n"
      "s_waitcnt vmcnt(1)\n"
      "s_nop 0\n"
      "v_mfma_f32_16x16x16_bf16 v[64:67], v[56:57], v[76:77], v[64:67]\n"
      "v_mfma_f32_16x16x16_bf16 v[68:71], v[58:59], v[76:77], v[68:71]\n"
      "v_and_b32_dpp v76, v81, v120 quad_perm:[3,3,3,3] row_mask:0xf bank_mask:0xf\n"
      "v_and_b32_dpp v77, v81, v121 quad_perm:[3,3,3,3] row_mask:0xf bank_mask:0xf\n"
      "s_waitcnt vmcnt(0)\n"
      "s_nop 0\n"
      "v_mfma_f32_16x16x16_bf16 v[64:67], v[60:61], v[76:77], v[64:67]\n"
      "v_mfma_f32_16x16x16_bf16 v[68:71], v[62:63], v[76:77], v[68:71]\n"
      "s_nop 7\n"
      "ds_write2_b32 v114, v64, v68 offset0:0 offset1:4\n"
      "ds_write2_b32 v114, v65, v69 offset0:8 offset1:12\n"
      "ds_write2_b32 v114, v66, v70 offset0:16 offset1:20\n"
      "ds_write2_b32 v114, v67, v71 offset0:24 offset1:28\n"
      "s_waitcnt lgkmcnt(0)\n"
      "s_barrier\n"
      "ds_read_b64 v[84:85], v115 offset:9856\n"
      "ds_read_b64 v[86:87], v115 offset:12320\n"
      "ds_read_b64 v[88:89], v115 offset:14784\n"
      "ds_read_b64 v[90:91], v115 offset:17248\n"
      "global_store_dword %[oo], v82, %[o]\n"
      "s_waitcnt lgkmcnt(3)\n"
      "v_mul_f32 v13, v20, v84\n"
      "v_mul_f32 v14, v20, v85\n"
      "s_waitcnt lgkmcnt(2)\n"
      "v_fmac_f32 v13, v21, v86\n"
      "v_fmac_f32 v14, v21, v87\n"
      "s_waitcnt lgkmcnt(1)\n"
      "v_fmac_f32 v13, v22, v88\n"
      "v_fmac_f32 v14, v22, v89\n"
      "s_waitcnt lgkmcnt(0)\n"
      "v_fmac_f32 v13, v23, v90\n"
      "v_fmac_f32 v14, v23, v91\n"
      "v_cvt_pk_bf16_f32 v83, v13, v14\n"
      "global_store_dword %[oo], v83, %[o] offset:1024\n"
      ".Lattn_end:\n"
      "s_mov_b32 m0, s36\n"
      : [ko] "+v"(ko), [cd] "+v"(cd)
      : [q] "s"(Q), [k] "s"(K), [v] "s"(V), [o] "s"(O), [so] "s"(so), [w] "s"(w), [b] "s"(b), [lds] "s"(lds), [tid] "v"(tid), [qo] "v"(qo), [oo] "v"(oo), [pc] "s"(pc), [co] "v"(co)
      : "memory", "scc", "s36", "s37", "s38", "s39", "s40", "s41", "s42", "s43", "s44", "s45", "s46", "s47", "s48", "s49", "s50", "s51", "s52", "s53", "s54", "s55", "s56", "s57", "s58", "s59", "s60", "s61", "s62", "s63", "s65", "s66", "s67", "s68", "s69", "s70", "s71", "s72", "s73", "s74", "s75", "s76", "s77", "s78", "s79", "s80", "s81", "s82", "s83", "s85", "s88", "s89", "s92", "s93", "s94", "s96",
        "v10", "v11", "v12", "v13", "v14", "v15", "v16", "v17", "v18", "v19", "v20", "v21", "v22", "v23", "v24", "v25", "v26", "v27", "v28", "v31", "v32", "v33", "v34", "v35", "v36", "v37", "v38", "v39", "v40", "v41", "v42", "v43", "v44", "v45", "v46", "v47", "v48", "v49", "v50", "v51", "v52", "v53", "v54", "v55", "v56", "v57", "v58", "v59", "v60", "v61", "v62", "v63", "v64", "v65", "v66", "v67", "v68", "v69", "v70", "v71", "v72", "v73", "v74", "v75", "v76", "v77", "v78", "v79", "v80", "v81", "v82", "v83", "v84", "v85", "v86", "v87", "v88", "v89", "v90", "v91", "v92", "v93", "v94", "v95", "v100", "v101", "v102", "v103", "v104", "v105", "v106", "v107", "v108", "v109", "v110", "v111", "v112", "v113", "v114", "v115", "v116", "v117", "v118", "v119", "v120", "v121", "v124", "v125", "v126", "v127", "v128", "v129", "v130", "v131", "v132", "v133", "v134", "v135", "v136", "v137", "v138", "v139", "v140", "v141", "v142", "v143", "v144", "v145", "v146", "v147", "v148", "v149", "v150", "v151", "v152", "v153", "v154", "v155");
}

extern "C" void kernel_launch(void* const* d_in, const int* in_sizes, int n_in, void* d_out, int out_size, void* d_ws, size_t ws_size,
                              hipStream_t stream) {
  attn_kernel<<<NWG, 256, 0, stream>>>((const __hip_bfloat16*)d_in[0], (const __hip_bfloat16*)d_in[1], (const __hip_bfloat16*)d_in[2],
                                       (__hip_bfloat16*)d_out);
}
```
